```python
import torch

def _softmax(x, axis=-1):
    return torch.softmax(x, dim=axis)
def _gelu(x):
    return torch.nn.functional.gelu(x, approximate="tanh")
def _silu(x):
    return torch.nn.functional.silu(x)
def _sigmoid(x):
    return torch.sigmoid(x)
def _layernorm(x, axis=-1, eps=1e-5):
    m = x.mean(dim=axis, keepdim=True)
    v = x.var(dim=axis, keepdim=True, unbiased=False)
    return (x - m) / torch.sqrt(v + eps)
def _scatter_add(tgt, idx, src, axis=0):
    return tgt.clone().index_add_(axis, idx, src)
def _segment_sum(src, seg, n):
    out = torch.zeros((n,) + src.shape[1:], dtype=src.dtype)
    return out.index_add_(0, seg, src)
def _pad(x, pw):
    flat = tuple(v for pair in reversed(pw) for v in pair)
    return torch.nn.functional.pad(x, flat)

kernel_name = 'concat_heads__ssm_conv_s4d__linear_attn_block__prompt'

def setup_inputs():

    _w = (lambda a: (lambda t: t.to(torch.bfloat16) if t.is_floating_point() else t)(torch.as_tensor(a)))
    def _A_setup():
        Bsz, L, D, N = 1, 2048, 576, 128
        return {
            "u": _w((lambda *s: torch.randn(*s, dtype=torch.bfloat16) * 0.05)(Bsz, L, D)),
            "log_A_real": _w((lambda *s: torch.randn(*s, dtype=torch.bfloat16) * 6.4)(D, N) * 0.5),
            "A_imag": _w((lambda *s: torch.randn(*s, dtype=torch.bfloat16) * 0.05)(D, N) * 3.14),
            "C_real": _w((lambda *s: torch.randn(*s, dtype=torch.bfloat16) * 0.1)(D, N) / N ** 0.5),
            "C_imag": _w((lambda *s: torch.randn(*s, dtype=torch.bfloat16) * 0.1)(D, N) / N ** 0.5),
            "log_dt": _w((lambda *s: torch.randn(*s, dtype=torch.bfloat16) * 0.05)(D) * 0.5 - 4.0),
            "D_skip": _w((lambda *s: torch.randn(*s, dtype=torch.bfloat16) * 0.05)(D)),
        }
    _A_in = _A_setup()

    def _B_setup():
        B, S, D = 1, 2048, 576
        return {
            "x": _w((lambda *s: torch.randn(*s, dtype=torch.bfloat16) * 0.05)(B, S, D)),
            "wqkv": _w((lambda *s: torch.randn(*s, dtype=torch.bfloat16) * 0.4)(D, 3 * D)),
            "wo": _w((lambda *s: torch.randn(*s, dtype=torch.bfloat16) * 0.05)(D, D)),
        }
    _B_in = _B_setup()

    return {
        "A_u": _A_in["u"],
        "A_log_A_real": _A_in["log_A_real"],
        "A_A_imag": _A_in["A_imag"],
        "A_C_real": _A_in["C_real"],
        "A_C_imag": _A_in["C_imag"],
        "A_log_dt": _A_in["log_dt"],
        "A_D_skip": _A_in["D_skip"],
        "B_x": _B_in["x"],
        "B_wqkv": _B_in["wqkv"],
        "B_wo": _B_in["wo"],
    }

def reference(A_u, A_log_A_real, A_A_imag, A_C_real, A_C_imag, A_log_dt, A_D_skip, B_x, B_wqkv, B_wo):
    def _A_ref(u, log_A_real, A_imag, C_real, C_imag, log_dt, D_skip):
        L = u.shape[1]
        dt = torch.exp(log_dt)
        A = -torch.exp(log_A_real) + 1j * A_imag
        C = (C_real + 1j * C_imag) * dt[:, None]
        pos = (lambda x, dt: x.to(dt))(torch.arange(L), log_dt.dtype)
        P = torch.exp((A * dt[:, None])[:, :, None] * pos[None, None, :])
        K = 2.0 * torch.einsum("dn,dnl->dl", C, P).real
        L2 = 2 * L
        u_t = (lambda x, *d: x.permute(*d))(u, 0, 2, 1)
        Kf = (lambda x, *a, **k: torch.fft.rfft(x.float(), *a, **k))(K, L2)
        Uf = (lambda x, *a, **k: torch.fft.rfft(x.float(), *a, **k))(u_t, L2)
        y = (lambda x, *a, **k: torch.fft.irfft(x, *a, **k).to(torch.bfloat16))(Uf * Kf[None, :, :], L2)[:, :, :L]
        y = (lambda x, *d: x.permute(*d))(y, 0, 2, 1)
        return y + u * D_skip[None, None, :]

    def _B_ref(x, wqkv, wo):
        B, S, D = x.shape
        H = 32; Dh = D // H
        qkv = (lambda x, *d: x.permute(*d))((x @ wqkv).reshape(B, S, 3, H, Dh), 2, 0, 3, 1, 4)
        q, k, v = qkv[0], qkv[1], qkv[2]
        phi = lambda t: torch.where(t > 0, t + 1, torch.exp(t)) + 1e-6
        pq, pk = phi(q), phi(k)
        kv = torch.einsum("bhsd,bhse->bhde", pk, v)
        z = torch.einsum("bhsd->bhd", pk)
        num = torch.einsum("bhqd,bhde->bhqe", pq, kv)
        den = torch.einsum("bhqd,bhd->bhq", pq, z)[..., None] + 1e-6
        o = num / den
        return (lambda x, *d: x.permute(*d))(o, 0, 2, 1, 3).reshape(B, S, D) @ wo

    a = _A_ref(A_u, A_log_A_real, A_A_imag, A_C_real, A_C_imag, A_log_dt, A_D_skip)
    b = _B_ref(B_x, B_wqkv, B_wo)
    return torch.concatenate([a, b], axis=-1)
```

```cpp
#include <hip/hip_runtime.h>
#include <hip/hip_bf16.h>
#include <stdint.h>
typedef unsigned short u16;
typedef short s4 __attribute__((ext_vector_type(4)));
typedef short s8 __attribute__((ext_vector_type(8)));
typedef __bf16 b8 __attribute__((ext_vector_type(8)));
typedef __bf16 b2 __attribute__((ext_vector_type(2)));
typedef _Float16 h8 __attribute__((ext_vector_type(8)));
typedef _Float16 h2 __attribute__((ext_vector_type(2)));
typedef float f4 __attribute__((ext_vector_type(4)));
typedef float f2 __attribute__((ext_vector_type(2)));
typedef unsigned u4 __attribute__((ext_vector_type(4)));
typedef unsigned u2 __attribute__((ext_vector_type(2)));
#define LAS __attribute__((address_space(3)))
#define GAS __attribute__((address_space(1)))
#define DI __device__ __forceinline__
typedef GAS unsigned gu32;
#define RLX_AGENT __ATOMIC_RELAXED, __HIP_MEMORY_SCOPE_AGENT
#define SCHED() __builtin_amdgcn_sched_barrier(0)
#define TS(i)
#define TSDECL int g_tsv
#define TSINIT int g_tsv=0
#define TSFLUSH

DI unsigned pk_bf16(float a, float b){ f2 v={a,b}; b2 r=__builtin_convertvector(v,b2); return __builtin_bit_cast(unsigned,r); }
DI unsigned pk_f16(float a,float b){ f2 v={a,b}; h2 r=__builtin_convertvector(v,h2); return __builtin_bit_cast(unsigned,r); }
DI float bflo(unsigned u){ return __uint_as_float(u<<16); }
DI float bfhi(unsigned u){ return __uint_as_float(u&0xffff0000u); }
DI float rbf1(float a){ return bflo(pk_bf16(a,a)); }
DI f4 mfma16(s8 a, s8 b, f4 c){ return __builtin_amdgcn_mfma_f32_16x16x32_bf16(__builtin_bit_cast(b8,a),__builtin_bit_cast(b8,b),c,0,0,0); }
DI s8 u4s8(u4 v){ return __builtin_bit_cast(s8,v); }
DI s4 trd(LAS const char* p){ return __builtin_amdgcn_ds_read_tr16_b64_v4i16((LAS s4*)p); }
DI s8 cat(s4 lo,s4 hi){ return __builtin_shufflevector(lo,hi,0,1,2,3,4,5,6,7); }
DI s8 trfrag(LAS const char* p, int rowbytes8){ return cat(trd(p),trd(p+rowbytes8)); }
DI void glds16(unsigned voff, const void* sbase, unsigned ldsaddr){
  asm volatile("s_mov_b32 m0, %2\n\ts_nop 0\n\tglobal_load_lds_dwordx4 %0, %1" :: "v"(voff), "s"(sbase), "s"(ldsaddr) : "memory");
}
DI unsigned phi2(float a, float b, float& tmin){
  unsigned u=pk_bf16(a,b); float ta=bflo(u), tb=bfhi(u);
  float ea=__builtin_amdgcn_fmed3f(__builtin_amdgcn_exp2f(ta*1.4426950408889634f),0.f,1.f);
  float eb=__builtin_amdgcn_fmed3f(__builtin_amdgcn_exp2f(tb*1.4426950408889634f),0.f,1.f);
  tmin=fminf(tmin,fminf(ta,tb));
  return pk_bf16(fmaxf(ta,0.f)+ea, fmaxf(tb,0.f)+eb);
}
DI unsigned addeps2(unsigned r){ return pk_bf16(bflo(r)+1e-6f, bfhi(r)+1e-6f); }

constexpr size_t WS_GEN=0, WS_STARTED=256, WS_FLAG1=4096, WS_FLAG2=20480, WS_FLAGQ=36864, WS_PART=65536;
constexpr int PART_STRIDE=6144;
constexpr size_t WS_OFRAG=1u<<20;
constexpr size_t WS_PQ=4u<<20;
constexpr int FS=32;
constexpr int LDS_TOTAL=163840;
constexpr unsigned OOB=0x80000000u;

struct Args { const u16* u; const u16* logA; const u16* Aim; const u16* Cre; const u16* Cim; const u16* logdt; const u16* Dsk;
              const u16* x; const u16* wqkv; const u16* wo; u16* out; char* ws; };

template<int V> struct IC { static constexpr int v=V; };
template<int N,class F> DI void sfor(F&& f){ if constexpr(N>0){ sfor<N-1>(f); f(IC<N-1>{}); } }

DI unsigned mktag(unsigned gen){ asm volatile("":"+v"(gen)); return gen+1u; }
DI void poll_flags(gu32* p, unsigned tag){
  for(unsigned it=0; it<(1u<<20); ++it){
    unsigned v=__hip_atomic_load(p, RLX_AGENT);
    if(__all(v==tag)) break;
    __builtin_amdgcn_s_sleep(1);
  }
}
#define WAITB(N) asm volatile("s_waitcnt vmcnt(" #N ") lgkmcnt(0)\n\ts_barrier":::"memory")
#define LBAR() asm volatile("s_waitcnt lgkmcnt(0)\n\ts_barrier":::"memory")

constexpr int WROW72=160, WCH72=64*WROW72, L_W72=40960;
DI void load_w72(const char* wbase, int ldw, int cb, int w, int lane, unsigned ldsbase){
  auto voff=[&](int n){ int P=64*n+lane; int rho=(P*6554)>>16; int sl=P-rho*10; int k=(rho&~12)|((rho&4)<<1)|((rho&8)>>1); if(sl==9) sl=8; return (unsigned)(k*ldw+cb+16*sl); };
  unsigned vo0=voff(w), vo1=voff(w<2?w+8:w);
  #pragma unroll
  for(int j=0;j<9;j++){
    const char* base=wbase+(size_t)j*64*ldw; unsigned l=ldsbase+L_W72+j*WCH72;
    glds16(vo0,base,l+w*1024);
    if(w<2) glds16(vo1,base,l+(w+8)*1024);
  }
}

DI void g2_stage(const Args& a, char* smem, int r64, int cg, unsigned tag, bool loaded, TSDECL){
  int tid=threadIdx.x; asm volatile("":"+v"(tid));
  const int lane=tid&63; const int w=__builtin_amdgcn_readfirstlane(tid>>6);
  const unsigned ldsbase=(unsigned)(size_t)(LAS char*)smem;
  if(!loaded) load_w72((const char*)a.wo,1152,144*cg,w,lane,ldsbase);
  const int mw=w&3, kh=w>>2;
  const int g=lane>>4,q=(lane&15)>>2,p=lane&3;
  const __amdgpu_buffer_rsrc_t rs=__builtin_amdgcn_make_buffer_rsrc((void*)(a.ws+WS_OFRAG),0,128*18*1024,0x00020000);
  const unsigned obase=((4*r64+mw)*18+9*kh)*1024+lane*16;
  WAITB(0);
  s8 wf[9][5];
  {
    LAS const char* base=(LAS const char*)smem+L_W72+(16*(g>>1)+4*(g&1)+q)*WROW72+8*p+kh*(9*32*WROW72);
    #pragma unroll
    for(int i=0;i<9;i++)
      #pragma unroll
      for(int c=0;c<5;c++) wf[i][c]=trfrag(base+i*32*WROW72+32*c,8*WROW72);
  }
  TS(8);
  if(w==0) poll_flags((gu32*)(a.ws+WS_FLAG2)+((r64>>1)*8+(lane&7))*FS,tag);
  LBAR();
  TS(9);
  u4 of[9];
  #pragma unroll
  for(int i=0;i<9;i++) of[i]=__builtin_amdgcn_raw_buffer_load_b128(rs,obase+i*1024,0,16);
  f4 acc[5];
  #pragma unroll
  for(int c=0;c<5;c++) acc[c]=f4{0,0,0,0};
  #pragma unroll
  for(int i=0;i<9;i++)
    #pragma unroll
    for(int c=0;c<5;c++) acc[c]=mfma16(wf[i][c],u4s8(of[i]),acc[c]);
  TS(10);
  LAS f4* red=(LAS f4*)smem;
  if(kh==1){
    #pragma unroll
    for(int c=0;c<5;c++) red[(mw*5+c)*64+lane]=acc[c];
  }
  LBAR();
  if(kh==0){
    const int s=lane&15,h=lane>>4;
    u16* orow=a.out+(size_t)(64*r64+16*mw+s)*1152+576+72*cg+4*h;
    #pragma unroll
    for(int c=0;c<5;c++){
      f4 v=acc[c]+red[(mw*5+c)*64+lane];
      u2 pk={pk_bf16(v[0],v[1]),pk_bf16(v[2],v[3])};
      if(c<4 || h<2) *(u2*)(orow+16*c)=pk;
    }
  }
}

constexpr int XCH=16384, KV_WCH=64*288, KV_SLOT=XCH+KV_WCH, KV_S0=24576;
constexpr int L_IMGZ=12288;

DI void kv_block(const Args& a, char* smem, int xcd, int idx, unsigned gen, TSDECL){
  const int tid=threadIdx.x, lane=tid&63; const int w=__builtin_amdgcn_readfirstlane(tid>>6);
  const int rb=(xcd&3)*4+(idx&3), hq=(xcd>>2)*4+(idx>>2);
  int rot=(idx>>2)*2+(idx&3); if(rot>=9) rot-=9;
  const int mw=w&3, nw=w>>2;
  const unsigned ldsbase=(unsigned)(size_t)(LAS char*)smem;
  const char* xb=(const char*)a.x; const char* wb=(const char*)a.wqkv;
  const unsigned xv0=(unsigned)((rb*128+16*w+(lane>>3))*1152+(((lane&7)^((lane>>3)&7))*16)), xv1=xv0+8*1152;
  auto kcof=[&](int c){ int kc=c+rot; if(kc>=9) kc-=9; return kc; };
  auto issueX=[&](int c,int i){ glds16(i?xv1:xv0,xb+kcof(c)*128,ldsbase+KV_S0+(c&3)*KV_SLOT+w*2048+i*1024); };
  issueX(0,0); issueX(0,1);
  auto kvoff=[&](int n)->unsigned{
    int half=n>=9, pat=n-9*half; int P=64*pat+lane; int rho=(P*3641)>>16, sl=P-rho*18;
    int k=(rho&~12)|((rho&4)<<1)|((rho&8)>>1);
    int cb = sl<9 ? 1152+16*sl : 2304-144+16*sl;
    return (k+32*half)*3456+cb+144*hq;
  };
  const unsigned wv0=kvoff(w), wv1=kvoff(w+8), wv2=kvoff(w<2?w+16:w);
  auto issueW=[&](int c,int i){
    const char* ws_=wb+(size_t)kcof(c)*64*3456; unsigned l=ldsbase+KV_S0+(c&3)*KV_SLOT+XCH+w*1024;
    if(i==0) glds16(wv0,ws_,l); else if(i==1) glds16(wv1,ws_,l+8192); else if(w<2) glds16(wv2,ws_,l+16384);
  };
  auto issueK=[&](int c){ issueX(c,0); issueX(c,1); issueW(c,0); issueW(c,1); issueW(c,2); };
  issueW(0,0); issueW(0,1); issueW(0,2);
  issueK(1); issueK(2);

  const int g=lane>>4,q=(lane&15)>>2,p=lane&3, r16=lane&15, h4=lane>>4;
  LAS const char* sm=(LAS const char*)smem;
  LAS const char* xr0=sm+KV_S0+(32*mw+r16)*128+((h4)^(r16&7))*16;
  LAS const char* xr1=sm+KV_S0+(32*mw+r16)*128+((4+h4)^(r16&7))*16;
  LAS const char* wr=sm+KV_S0+XCH+(16*(g>>1)+4*(g&1)+q)*288+72*nw+8*p;
  LAS const char* wr4=sm+KV_S0+XCH+(16*(g>>1)+4*(g&1)+q)*288+72*nw+(p==0?64:(p==1?208:(p==2?72:24)));
  const __amdgpu_buffer_rsrc_t rsP=__builtin_amdgcn_make_buffer_rsrc((void*)(a.ws+WS_PART),0,128*PART_STRIDE,0x00020000);
  const int fa0= mw==0?0:(mw==3?4:1), fb0= mw<2?2:(mw==2?4:3);
  const int fa1= mw==1?1:(mw==3?4:0), fb1= mw<2?3:4;
  unsigned po[4];
  {
    const unsigned pb=(rb*8+hq)*PART_STRIDE+nw*760*4;
    auto mk=[&](int fa,int fb,int i)->unsigned{
      int kb= fa==4?32:16*fa; bool isz=(fb==4 && r16==8);
      int vc= fb==4 ? (28+r16) : (16*(fb-2)+r16); bool vok= fb==4 ? (r16>=4 && r16<8) : true; bool hv=vc>=18;
      int kc=kb+4*h4+2*i; bool hk=kc>=18; int d=kc-(hk?18:0);
      bool ok= kc<36 && !(fa==4 && h4>0) && (isz || (vok && hk==hv));
      int id=(isz ? 720+(hk?20:0) : vc*20)+d;
      return ok? pb+id*4 : OOB;
    };
    po[0]=mk(fa0,fb0,0); po[1]=mk(fa0,fb0,1); po[2]=mk(fa1,fb1,0); po[3]=mk(fa1,fb1,1);
  }
  s8 xf[2][2]; s4 bl[2][5], bh[2][5];
  f4 acc[2][5];
  #pragma unroll
  for(int m=0;m<2;m++)
  #pragma unroll
  for(int c=0;c<5;c++) acc[m][c]=f4{0,0,0,0};
  auto rd1=[&](auto S,auto I){
    constexpr int s=decltype(S)::v, i=decltype(I)::v; constexpr int c=s>>1, s2=s&1; constexpr int so=(c&3)*KV_SLOT; constexpr int wo_=so+s2*32*288;
    if constexpr(i==0) xf[s&1][0]=*(LAS const s8*)((s2?xr1:xr0)+so);
    else if constexpr(i==1) xf[s&1][1]=*(LAS const s8*)((s2?xr1:xr0)+so+16*128);
    else { constexpr int t=(i-2)>>1; constexpr int hi=(i-2)&1; constexpr int co= t==0?0:(t==1?32:(t==2?144:176));
      LAS const char* pp=(t==4? wr4 : wr+co)+wo_+hi*8*288;
      if constexpr(hi) bh[s&1][t]=trd(pp); else bl[s&1][t]=trd(pp); }
  };
  WAITB(8); TS(1);
  sfor<12>([&](auto I){ rd1(IC<0>{},I); });
  sfor<18>([&](auto S){
    constexpr int s=decltype(S)::v; constexpr int c=s>>1;
    if constexpr((s&1) && c<8){ if constexpr(c<=5) WAITB(8); else if constexpr(c==6) WAITB(4); else WAITB(0); }
    SCHED();
    sfor<10>([&](auto I){
      constexpr int i=decltype(I)::v; constexpr int t=i>>1, m=i&1;
      acc[m][t]=mfma16(xf[s&1][m],cat(bl[s&1][t],bh[s&1][t]),acc[m][t]);
      if constexpr(s<17){
        if constexpr(i<2){ rd1(IC<s+1>{},IC<2*i>{}); rd1(IC<s+1>{},IC<2*i+1>{}); }
        else rd1(IC<s+1>{},IC<i+2>{});
      }
      constexpr int cn= s==0 ? 3 : ((s&1) ? c+4 : 99);
      if constexpr(cn<=8){
        if constexpr(i==1) issueX(cn,0); else if constexpr(i==3) issueX(cn,1);
        else if constexpr(i==5) issueW(cn,0); else if constexpr(i==7) issueW(cn,1); else if constexpr(i==9) issueW(cn,2);
      }
      SCHED();
    });
  });
  TS(2);
  {
    float tmin=0.f;
    unsigned pk[5][4];
    #pragma unroll
    for(int m=0;m<2;m++){
      #pragma unroll
      for(int j=0;j<2;j++){
        pk[0][2*m+j]=phi2(acc[m][0][2*j],acc[m][0][2*j+1],tmin);
        pk[1][2*m+j]=phi2(acc[m][1][2*j],acc[m][1][2*j+1],tmin);
        pk[2][2*m+j]=pk_bf16(acc[m][2][2*j],acc[m][2][2*j+1]);
        pk[3][2*m+j]=pk_bf16(acc[m][3][2*j],acc[m][3][2*j+1]);
        float t4=0.f;
        unsigned ph=phi2(acc[m][4][2*j],acc[m][4][2*j+1],t4);
        unsigned pl=pk_bf16(acc[m][4][2*j],acc[m][4][2*j+1]);
        tmin=fminf(tmin, r16<4? t4:0.f);
        pk[4][2*m+j]= r16<4 ? ph : pl;
      }
    }
    if(__any(tmin<-6.9f)){
      #pragma unroll
      for(int i=0;i<4;i++){ pk[0][i]=addeps2(pk[0][i]); pk[1][i]=addeps2(pk[1][i]); if(r16<4) pk[4][i]=addeps2(pk[4][i]); }
    }
    #pragma unroll
    for(int i=0;i<4;i++) pk[4][i]= r16<8 ? pk[4][i] : (r16==8 ? 0x3f803f80u : 0u);
    LBAR();
    LAS u4* fx=(LAS u4*)smem+w*320+lane;
    #pragma unroll
    for(int t=0;t<5;t++) fx[t*64]=u4{pk[t][0],pk[t][1],pk[t][2],pk[t][3]};
    LBAR();
    f4 P0={0,0,0,0}, P1={0,0,0,0};
    LAS const s8* fr=(LAS const s8*)smem+nw*4*320+lane;
    #pragma unroll
    for(int mm=0;mm<4;mm++){
      s8 A0=fr[mm*320+fa0*64], B0=fr[mm*320+fb0*64], A1=fr[mm*320+fa1*64], B1=fr[mm*320+fb1*64];
      P0=mfma16(A0,B0,P0); P1=mfma16(A1,B1,P1);
    }
    __builtin_amdgcn_raw_buffer_store_b64(u2{__float_as_uint(P0[0]),__float_as_uint(P0[1])},rsP,po[0],0,16);
    __builtin_amdgcn_raw_buffer_store_b64(u2{__float_as_uint(P0[2]),__float_as_uint(P0[3])},rsP,po[1],0,16);
    __builtin_amdgcn_raw_buffer_store_b64(u2{__float_as_uint(P1[0]),__float_as_uint(P1[1])},rsP,po[2],0,16);
    __builtin_amdgcn_raw_buffer_store_b64(u2{__float_as_uint(P1[2]),__float_as_uint(P1[3])},rsP,po[3],0,16);
  }
  WAITB(0);
  const unsigned tag=mktag(gen);
  if(tid==0) __hip_atomic_store((gu32*)(a.ws+WS_FLAG1)+(hq*16+rb)*FS,tag,RLX_AGENT);
  TS(3);
  int tid2=threadIdx.x; asm volatile("":"+v"(tid2));
  const int ln=tid2&63, s16=ln&15, hh=ln>>4;
  load_w72((const char*)a.wo,1152,144*(idx>>2),w,ln,ldsbase);
  for(int i=tid2;i<800;i+=512) *((LAS u4*)smem+i)=u4{0,0,0,0};
  const int cn=tid2>=190, cr=tid2-190*cn;
  const bool cz=cr>=180; const int crr=cz?cr-180:cr;
  const int cec=(crr*13108)>>16, cd0=4*(crr-5*cec);
  const int chl= cz? cec : (cec>=18);
  const int dc0=18*chl+cd0, dc1=dc0+2;
  const int irow= cz? chl : cec;
  const unsigned ibase=(cz? L_IMGZ+cn*256 : cn*6144)+irow*128;
  const unsigned ia0=ibase+(((dc0>>3)^(irow&7))*16)+(dc0&7)*2, ia1=ibase+(((dc1>>3)^(irow&7))*16)+(dc1&7)*2;
  const __amdgpu_buffer_rsrc_t rsQ=__builtin_amdgcn_make_buffer_rsrc((void*)(a.ws+WS_PQ),0,128*16*2048,0x00020000);
  const unsigned qo=((rb*8+2*mw)*16+2*hq+nw)*2048;
  if(w==0){
    gu32* fp= ln<16 ? (gu32*)(a.ws+WS_FLAG1)+(hq*16+ln)*FS : (gu32*)(a.ws+WS_FLAGQ)+(rb*4+(ln<19?ln-16:0))*FS;
    poll_flags(fp,tag);
  }
  LBAR();
  TS(5);
  s8 pf[2][2];
  {
    u4 p0=__builtin_amdgcn_raw_buffer_load_b128(rsQ,qo+ln*16,0,16), p1=__builtin_amdgcn_raw_buffer_load_b128(rsQ,qo+16*2048+ln*16,0,16);
    unsigned o2= hh==0 ? qo+1024+s16*16 : OOB;
    u2 t0=__builtin_amdgcn_raw_buffer_load_b64(rsQ,o2,0,16), t1=__builtin_amdgcn_raw_buffer_load_b64(rsQ,hh==0?o2+16*2048:OOB,0,16);
    if(tid2<380){
      u4 pv[16];
      #pragma unroll
      for(int r=0;r<16;r++) pv[r]=__builtin_amdgcn_raw_buffer_load_b128(rsP,(r*8+hq)*PART_STRIDE+tid2*16,0,16);
      f4 s0=__builtin_bit_cast(f4,pv[0]), s1=__builtin_bit_cast(f4,pv[1]);
      #pragma unroll
      for(int r=2;r<16;r+=2){ s0+=__builtin_bit_cast(f4,pv[r]); s1+=__builtin_bit_cast(f4,pv[r+1]); }
      s0+=s1;
      *(LAS unsigned*)(smem+ia0)=pk_bf16(s0[0],s0[1]);
      if(cd0<16) *(LAS unsigned*)(smem+ia1)=pk_bf16(s0[2],s0[3]);
    }
    pf[0][0]=u4s8(p0); pf[1][0]=u4s8(p1);
    pf[0][1]=u4s8(u4{t0[0],t0[1],0u,0u}); pf[1][1]=u4s8(u4{t1[0],t1[1],0u,0u});
  }
  LBAR();
  {
    const __amdgpu_buffer_rsrc_t rsO=__builtin_amdgcn_make_buffer_rsrc((void*)(a.ws+WS_OFRAG),0,128*18*1024,0x00020000);
    s8 an[3][2],ad[3][2];
    #pragma unroll
    for(int c=0;c<3;c++){
      int row=16*c+s16; int zr= row>=18;
      #pragma unroll
      for(int kp=0;kp<2;kp++){
        an[c][kp]=*(LAS const s8*)(sm+nw*6144+row*128+(((4*kp+hh)^(s16&7))*16));
        ad[c][kp]=*(LAS const s8*)(sm+L_IMGZ+nw*256+zr*128+(((4*kp+hh)^zr)*16));
      }
    }
    f4 num[2][3],den[2][3]; float dmin=1e30f;
    #pragma unroll
    for(int m=0;m<2;m++)
    #pragma unroll
    for(int c=0;c<3;c++){
      num[m][c]=mfma16(an[c][1],pf[m][1],mfma16(an[c][0],pf[m][0],f4{0,0,0,0}));
      den[m][c]=mfma16(ad[c][1],pf[m][1],mfma16(ad[c][0],pf[m][0],f4{0,0,0,0}));
    }
    unsigned db[2][3][2];
    #pragma unroll
    for(int m=0;m<2;m++)
    #pragma unroll
    for(int c=0;c<3;c++)
    #pragma unroll
    for(int j=0;j<2;j++){ db[m][c][j]=pk_bf16(den[m][c][2*j],den[m][c][2*j+1]); dmin=fminf(dmin,fminf(den[m][c][2*j],den[m][c][2*j+1])); }
    if(__any(!(dmin>=0.002f))){
      #pragma unroll
      for(int m=0;m<2;m++)
      #pragma unroll
      for(int c=0;c<3;c++)
      #pragma unroll
      for(int j=0;j<2;j++) db[m][c][j]=addeps2(db[m][c][j]);
    }
    #pragma unroll
    for(int c=0;c<3;c++){
      int K=72*hq+36*nw+16*c+4*hh; int ks=K>>5, kk=K&31;
      unsigned off0=(((rb*8+2*mw)*18+ks)*64+(kk>>3)*16+s16)*16+(kk&7)*2;
      if(c==2 && hh>0) off0=OOB;
      #pragma unroll
      for(int m=0;m<2;m++){
        unsigned o[2];
        #pragma unroll
        for(int j=0;j<2;j++){
          unsigned nb=pk_bf16(num[m][c][2*j],num[m][c][2*j+1]);
          o[j]=pk_bf16(bflo(nb)*__builtin_amdgcn_rcpf(bflo(db[m][c][j])), bfhi(nb)*__builtin_amdgcn_rcpf(bfhi(db[m][c][j])));
        }
        __builtin_amdgcn_raw_buffer_store_b64(u2{o[0],o[1]},rsO,off0+m*18*1024,0,16);
      }
    }
  }
  WAITB(0);
  if(tid2==0) __hip_atomic_store((gu32*)(a.ws+WS_FLAG2)+(rb*8+hq)*FS,tag,RLX_AGENT);
  TS(6);
}

constexpr int Q_WCH=64*384, Q_SLOT=XCH+Q_WCH;
DI void q_block(const Args& a, char* smem, int rb, int cgq, unsigned gen, TSDECL){
  const int tid=threadIdx.x, lane=tid&63; const int w=__builtin_amdgcn_readfirstlane(tid>>6);
  const int mw=w&3, nw=w>>2;
  const int rot=3*cgq;
  const unsigned ldsbase=(unsigned)(size_t)(LAS char*)smem;
  const char* xb=(const char*)a.x; const char* wb=(const char*)a.wqkv;
  const unsigned xv0=(unsigned)((rb*128+16*w+(lane>>3))*1152+(((lane&7)^((lane>>3)&7))*16)), xv1=xv0+8*1152;
  auto kcof=[&](int c){ int kc=c+rot; if(kc>=9) kc-=9; return kc; };
  unsigned wv[3];
  #pragma unroll
  for(int i=0;i<3;i++){
    int P=64*i+lane; int r=(P*2731)>>16, sl=P-r*24; int rho=8*w+r;
    int k=(rho&~12)|((rho&4)<<1)|((rho&8)>>1);
    wv[i]=k*3456+384*cgq+16*(sl^(((rho>>1)&3)<<1));
  }
  auto issue1=[&](int c,int i){
    unsigned l=ldsbase+(c&3)*Q_SLOT;
    if(i<2) glds16(i?xv1:xv0,xb+kcof(c)*128,l+w*2048+i*1024);
    else glds16(wv[i-2],wb+(size_t)kcof(c)*64*3456,l+XCH+w*3072+(i-2)*1024);
  };
  #pragma unroll
  for(int c=0;c<3;c++)
  #pragma unroll
  for(int i=0;i<5;i++) issue1(c,i);
  const int g=lane>>4,q=(lane&15)>>2,p=lane&3, r16=lane&15, h4=lane>>4;
  LAS const char* sm=(LAS const char*)smem;
  LAS const char* xr0=sm+(32*mw+r16)*128+((h4)^(r16&7))*16;
  LAS const char* xr1=sm+(32*mw+r16)*128+((4+h4)^(r16&7))*16;
  const int xr=2*(g&1)+(q>>1);
  LAS const char* wrow=sm+XCH+(16*(g>>1)+4*(g&1)+q)*384+8*p;
  unsigned wco[6];
  #pragma unroll
  for(int t=0;t<6;t++) wco[t]=((6*nw+t)^xr)*32;
  s8 xf[2][2]; s4 bl[2][6], bh[2][6];
  f4 acc[2][6];
  #pragma unroll
  for(int m=0;m<2;m++)
  #pragma unroll
  for(int c=0;c<6;c++) acc[m][c]=f4{0,0,0,0};
  auto rd1=[&](auto S,auto I){
    constexpr int s=decltype(S)::v, i=decltype(I)::v; constexpr int c=s>>1, s2=s&1; constexpr int so=(c&3)*Q_SLOT; constexpr int wo_=so+s2*32*384;
    if constexpr(i==0) xf[s&1][0]=*(LAS const s8*)((s2?xr1:xr0)+so);
    else if constexpr(i==1) xf[s&1][1]=*(LAS const s8*)((s2?xr1:xr0)+so+16*128);
    else { constexpr int t=(i-2)>>1; constexpr int hi=(i-2)&1;
      LAS const char* pp=wrow+wco[t]+wo_+hi*8*384;
      if constexpr(hi) bh[s&1][t]=trd(pp); else bl[s&1][t]=trd(pp); }
  };
  WAITB(10); TS(1);
  sfor<14>([&](auto I){ rd1(IC<0>{},I); });
  sfor<18>([&](auto S){
    constexpr int s=decltype(S)::v; constexpr int c=s>>1;
    if constexpr((s&1) && c<8){ if constexpr(c<=5) WAITB(10); else if constexpr(c==6) WAITB(5); else WAITB(0); }
    SCHED();
    sfor<12>([&](auto I){
      constexpr int i=decltype(I)::v; constexpr int t=i>>1, m=i&1;
      acc[m][t]=mfma16(cat(bl[s&1][t],bh[s&1][t]),xf[s&1][m],acc[m][t]);
      if constexpr(s<17){
        if constexpr(i<2){ rd1(IC<s+1>{},IC<2*i>{}); rd1(IC<s+1>{},IC<2*i+1>{}); }
        else rd1(IC<s+1>{},IC<i+2>{});
      }
      constexpr int cn= s==0 ? 3 : ((s&1) ? c+4 : 99);
      if constexpr(cn<=8 && (i&1) && i<10) issue1(cn,i>>1);
      SCHED();
    });
  });
  TS(2);
  {
    float tmin=0.f; unsigned pk[2][6][2];
    #pragma unroll
    for(int m=0;m<2;m++)
    #pragma unroll
    for(int t=0;t<6;t++)
    #pragma unroll
    for(int j=0;j<2;j++) pk[m][t][j]=phi2(acc[m][t][2*j],acc[m][t][2*j+1],tmin);
    if(__any(tmin<-6.9f)){
      #pragma unroll
      for(int m=0;m<2;m++)
      #pragma unroll
      for(int t=0;t<6;t++)
      #pragma unroll
      for(int j=0;j<2;j++) pk[m][t][j]=addeps2(pk[m][t][j]);
    }
    const __amdgpu_buffer_rsrc_t rsQ=__builtin_amdgcn_make_buffer_rsrc((void*)(a.ws+WS_PQ),0,128*16*2048,0x00020000);
    #pragma unroll
    for(int t=0;t<6;t++){
      int col=192*cgq+96*nw+16*t+4*h4; int g36=(col*1821)>>16; int c36=col-36*g36;
      unsigned off=(((rb*8+2*mw)*16+g36)*2+(c36>>5))*1024+(((c36&31)>>3)*16+r16)*16+(c36&7)*2;
      __builtin_amdgcn_raw_buffer_store_b64(u2{pk[0][t][0],pk[0][t][1]},rsQ,off,0,16);
      __builtin_amdgcn_raw_buffer_store_b64(u2{pk[1][t][0],pk[1][t][1]},rsQ,off+16*2048,0,16);
    }
  }
  WAITB(0);
  if(tid==0) __hip_atomic_store((gu32*)(a.ws+WS_FLAGQ)+(rb*4+cgq)*FS,mktag(gen),RLX_AGENT);
  TS(3);
}

constexpr int AW=20480, A_K0=0, A_K1=4224, A_U0=8704, A_U1=13248, A_KC=17792, A_TROW=544;
struct C2 { f2 r,i; };
DI C2 cmul(const C2& a,const C2& b){ C2 o; o.r=a.r*b.r-a.i*b.i; o.i=a.r*b.i+a.i*b.r; return o; }
DI float exp_acc(float x){
  const float L2E=1.4426950408889634f, L2E_LO=1.925963033500011e-08f;
  float t=x*L2E; float er=__builtin_fmaf(x,L2E,-t)+x*L2E_LO;
  float e=__builtin_amdgcn_exp2f(t);
  return __builtin_fmaf(e*er,0.6931471805599453f,e);
}
DI C2 anchor(f2 lz,f2 rz,float p){
  f2 m=lz*p, an=rz*p; C2 o;
  float m0=__builtin_amdgcn_exp2f(m[0]), m1=__builtin_amdgcn_exp2f(m[1]);
  float a0=__builtin_amdgcn_fractf(an[0]), a1=__builtin_amdgcn_fractf(an[1]);
  o.r=f2{m0*__builtin_amdgcn_cosf(a0), m1*__builtin_amdgcn_cosf(a1)};
  o.i=f2{m0*__builtin_amdgcn_sinf(a0), m1*__builtin_amdgcn_sinf(a1)};
  return o;
}
DI unsigned wave_max_u32(unsigned v){
  v=max(v,(unsigned)__builtin_amdgcn_update_dpp(0,(int)v,0x111,0xf,0xf,false));
  v=max(v,(unsigned)__builtin_amdgcn_update_dpp(0,(int)v,0x112,0xf,0xf,false));
  v=max(v,(unsigned)__builtin_amdgcn_update_dpp(0,(int)v,0x114,0xf,0xf,false));
  v=max(v,(unsigned)__builtin_amdgcn_update_dpp(0,(int)v,0x118,0xf,0xf,false));
  v=max(v,(unsigned)__builtin_amdgcn_update_dpp(0,(int)v,0x142,0xa,0xf,false));
  v=max(v,(unsigned)__builtin_amdgcn_update_dpp(0,(int)v,0x143,0xc,0xf,false));
  return (unsigned)__builtin_amdgcn_readlane((int)v,63);
}

DI void a_block(const Args& a, char* smem, int ab, TSDECL){
  const int tid=threadIdx.x, lane=tid&63; const int w=__builtin_amdgcn_readfirstlane(tid>>6);
  const int d0=8*ab, d=d0+w;
  const unsigned pl=((const unsigned*)(a.logA+d*128))[lane], pa=((const unsigned*)(a.Aim+d*128))[lane];
  const unsigned pcr=((const unsigned*)(a.Cre+d*128))[lane], pci=((const unsigned*)(a.Cim+d*128))[lane];
  const float ldt=bflo(a.logdt[d]), dsk=bflo(a.Dsk[d]);
  u4 uv[4];
  {
    const char* ub=(const char*)a.u+(size_t)(4*tid)*1152+d0*2;
    #pragma unroll
    for(int i=0;i<4;i++) uv[i]=*(const u4*)(ub+i*1152);
  }
  LAS char* wb=(LAS char*)smem+w*AW;
  if(lane<28){ *(LAS u4*)(wb+A_U0+4096+lane*16)=u4{0,0,0,0}; *(LAS u4*)(wb+A_U1+4096+lane*16)=u4{0,0,0,0}; }
  if(lane==32) *(LAS unsigned*)(wb+A_U0+4092)=0;
  if(w<4) __builtin_amdgcn_s_setprio(2);
  const float dt=rbf1(exp_acc(ldt));
  f2 zr={-rbf1(exp_acc(bflo(pl)))*dt, -rbf1(exp_acc(bfhi(pl)))*dt};
  f2 zi={bflo(pa)*dt,bfhi(pa)*dt};
  C2 c0; c0.r=f2{bflo(pcr)*dt,bfhi(pcr)*dt}; c0.i=f2{bflo(pci)*dt,bfhi(pci)*dt};
  unsigned mx=max(max(__float_as_uint(c0.r[0])&0x7fffffffu,__float_as_uint(c0.r[1])&0x7fffffffu),max(__float_as_uint(c0.i[0])&0x7fffffffu,__float_as_uint(c0.i[1])&0x7fffffffu));
  mx=wave_max_u32(mx);
  int ex=(int)(mx>>23); ex= ex<30?30:(ex>230?230:ex);
  const float scA=__uint_as_float((unsigned)(14+127+127-ex)<<23);
  const float unsc=__uint_as_float((unsigned)(ex-27)<<23);
  c0.r*=scA; c0.i*=scA;
  const f2 lz=zr*1.4426950408889634f, rz=zi*0.15915494309189535f;
  const C2 w1=anchor(lz,rz,1.f), w8=anchor(lz,rz,8.f), w128=anchor(lz,rz,128.f), w1024=anchor(lz,rz,1024.f);
  {
    LAS char* p1=(LAS char*)smem+A_U1+(2044-4*tid)*2;
    LAS char* p0=(LAS char*)smem+A_U0+(2042-4*tid)*2;
    #pragma unroll
    for(int c=0;c<8;c++){
      const unsigned sel=(c&1)?0x07060302u:0x05040100u;
      unsigned dw0=__builtin_amdgcn_perm(uv[2][c>>1],uv[3][c>>1],sel), dw1=__builtin_amdgcn_perm(uv[0][c>>1],uv[1][c>>1],sel);
      *(LAS u2*)(p1+c*AW)=u2{dw0,dw1};
      *(LAS unsigned*)(p0+c*AW)=dw0; *(LAS unsigned*)(p0+c*AW+4)=dw1;
    }
  }
  LBAR();
  TS(1);
  LAS char* T=wb+lane*8;
  auto put=[&](int row,const C2& v){ *(LAS u2*)(T+row*A_TROW)=u2{pk_f16(v.r[0],v.i[0]),pk_f16(v.r[1],v.i[1])}; };
  LAS const char* TR=wb+(lane&15)*A_TROW+(lane>>4)*16;
  auto frag=[&](int s){ return *(LAS const h8*)(TR+s*64); };
  h8 Bf[8];
  {
    C2 v; v.r=f2{16384.f,16384.f}; v.i=f2{0.f,0.f};
    C2 w8c=w8; w8c.i=-w8.i;
    const C2 w16c=cmul(w8c,w8c);
    C2 vo; vo.r=w8c.r*16384.f; vo.i=w8c.i*16384.f;
    put(0,v); put(1,vo);
    #pragma unroll
    for(int j=2;j<16;j+=2){ v=cmul(v,w16c); vo=cmul(vo,w16c); put(j,v); put(j+1,vo); }
    #pragma unroll
    for(int s=0;s<8;s++) Bf[s]=frag(s);
  }
  f4 D[3];
  auto tile=[&](int t){ f4 acc={0,0,0,0};
    #pragma unroll
    for(int s=0;s<8;s++) acc=__builtin_amdgcn_mfma_f32_16x16x32_f16(frag(s),Bf[s],acc,0,0,0);
    D[t]=acc; };
  C2 c2,c4,c6;
  {
    C2 v=c0;
    #pragma unroll
    for(int i=0;i<8;i++){
      put(i,v); put(8+i,cmul(v,w128));
      if(i==2) c2=v; if(i==4) c4=v; if(i==6) c6=v;
      if(i<7) v=cmul(v,w1);
    }
    tile(0);
  }
  {
    const C2 w256=cmul(w128,w128), w512=cmul(w256,w256);
    C2 v;
    v=cmul(c0,w256); put(0,v); put(4,cmul(v,w128));
    v=cmul(c2,w256); put(1,v); put(5,cmul(v,w128));
    v=cmul(c4,w256); put(2,v); put(6,cmul(v,w128));
    v=cmul(c6,w256); put(3,v); put(7,cmul(v,w128));
    C2 p=cmul(c0,w512), q=cmul(c4,w512);
    #pragma unroll
    for(int i=0;i<4;i++){ put(8+2*i,p); put(9+2*i,q); if(i<3){ p=cmul(p,w128); q=cmul(q,w128);} }
    tile(1);
  }
  {
    C2 v=cmul(c0,w1024);
    #pragma unroll
    for(int i=0;i<9;i++){ put(i,v); if(i<8) v=cmul(v,w128); }
    tile(2);
  }
  TS(2);
  {
    const int j=lane&15,h=lane>>4;
    LAS char* kc=wb+A_KC;
    unsigned p01=pk_bf16(D[0][0]*unsc+(lane==0?dsk:0.f),D[0][1]*unsc), p23=pk_bf16(D[0][2]*unsc,D[0][3]*unsc);
    *(LAS u2*)(kc+2*(128*(h>>1)+4*(h&1)+8*j))=u2{p01,p23};
    p01=pk_bf16(D[1][0]*unsc,D[1][1]*unsc); p23=pk_bf16(D[1][2]*unsc,D[1][3]*unsc);
    if(h<2) *(LAS u2*)(kc+2*(256+64*h+4*j))=u2{p01,p23};
    else { *(LAS unsigned*)(kc+2*(384+64*(h-2)+2*j))=p01; *(LAS unsigned*)(kc+2*(384+64*(h-2)+32+2*j))=p23; }
    p01=pk_bf16(D[2][0]*unsc,D[2][1]*unsc); p23=pk_bf16(D[2][2]*unsc,D[2][3]*unsc);
    if(h<2){
      LAS u16* k16=(LAS u16*)(kc+2*(512+64*h+j));
      k16[0]=(u16)p01; k16[16]=(u16)(p01>>16); k16[32]=(u16)p23; k16[48]=(u16)(p23>>16);
    } else if(lane==32) *(LAS u16*)(kc+2*640)=(u16)p01;
  }
  {
    LAS const char* kc=wb+A_KC;
    if(lane<8){ *(LAS u4*)(wb+A_K0+lane*16)=u4{0,0,0,0}; *(LAS u4*)(wb+A_K1+lane*16)=u4{0,0,0,0}; }
    auto st=[&](int wi,u4 k0,u4 k1){ *(LAS u4*)(wb+A_K0+(8*wi+56)*2)=k0; *(LAS u4*)(wb+A_K1+(8*wi+56)*2)=k1; };
    {
      u4 k0,k1;
      if(lane<32){
        u4 v=*(LAS const u4*)(kc+16*lane);
        unsigned pv= lane>0 ? *(LAS const unsigned*)(kc+16*lane-4) : 0u;
        k1=v;
        k0=u4{__builtin_amdgcn_alignbit(v[0],pv,16),__builtin_amdgcn_alignbit(v[1],v[0],16),__builtin_amdgcn_alignbit(v[2],v[1],16),__builtin_amdgcn_alignbit(v[3],v[2],16)};
      } else {
        int i0=256+4*(lane-32);
        u2 v=*(LAS const u2*)(kc+2*i0); unsigned n=*(LAS const u16*)(kc+2*i0+8);
        unsigned pv=*(LAS const u16*)(kc+2*i0-2);
        k1=u4{__builtin_amdgcn_perm(v[0],v[0],0x01000100u),__builtin_amdgcn_alignbit(v[1],v[0],16),__builtin_amdgcn_perm(v[1],v[1],0x01000100u),__builtin_amdgcn_perm(n,v[1],0x05040302u)};
        unsigned first= lane==32 ? (pv|(v[0]<<16)) : k1[0];
        k0=u4{first,v[0],k1[2],v[1]};
      }
      st(lane,k0,k1);
    }
    {
      int i0=384+2*lane; unsigned ab_=*(LAS const unsigned*)(kc+2*i0); unsigned n=*(LAS const u16*)(kc+2*i0+4);
      unsigned aa=__builtin_amdgcn_perm(ab_,ab_,0x01000100u), bb=__builtin_amdgcn_perm(ab_,ab_,0x03020302u), bn=__builtin_amdgcn_perm(n,ab_,0x05040302u), nn=__builtin_amdgcn_perm(n,n,0x01000100u);
      st(64+lane,u4{aa,aa,bb,bn},u4{aa,ab_,bb,nn});
    }
    #pragma unroll
    for(int qq=0;qq<2;qq++){
      int i0=512+64*qq+lane; unsigned c=*(LAS const u16*)(kc+2*i0), n=*(LAS const u16*)(kc+2*i0+2);
      unsigned cc=c|(c<<16), nn=n|(n<<16); unsigned x=(lane&1)?n:c;
      st(128+64*qq+lane,u4{cc,cc,c|(x<<16),nn},u4{cc,cc,x|(n<<16),nn});
    }
  }
  __builtin_amdgcn_s_setprio(0);
  TS(3);
  f4 acc[8];
  #pragma unroll
  for(int g=0;g<8;g++) acc[g]=f4{0,0,0,0};
  {
    const int r=lane&15,h=lane>>4;
    const int c1=(r>>1)&1,c2_=r>>2;
    unsigned kaa=(unsigned)(size_t)(wb+((r&1)?A_K1:A_K0)+(8*((r>>1)-h)+24)*2);
    unsigned uaa=(unsigned)(size_t)(wb+((r&1)?A_U1:A_U0)+(2012-64*c2_-4*c1-8*h)*2)-7*512;
    #pragma unroll 1
    for(int tau=0;tau<8;tau++){
      s8 A[8]; s4 Bl[8],Bh[8];
      #pragma unroll
      for(int i=0;i<8;i++){
        A[i]=*(LAS const s8*)(size_t)(kaa+i*512);
        Bl[i]=*(volatile LAS s4*)(size_t)(uaa+(7-i)*512); Bh[i]=*(volatile LAS s4*)(size_t)(uaa+(7-i)*512+8);
      }
      #pragma unroll
      for(int e=0;e<8;e++){
        s8 B=cat(Bl[e],Bh[e]);
        #pragma unroll
        for(int i=0;i+e<8;i++) acc[i+e]=mfma16(A[i],B,acc[i+e]);
      }
      kaa+=64; uaa+=64;
    }
  }
  TS(4);
  {
    const int col=lane&15,h=lane>>4;
    LAS char* yp=wb+(16*h+2*(col&3)+64*(col>>2))*2;
    #pragma unroll
    for(int g=0;g<8;g++){
      #pragma unroll
      for(int k=0;k<2;k++) *(LAS unsigned*)(yp+(256*g+8*k)*2)=pk_bf16(acc[g][2*k],acc[g][2*k+1]);
    }
  }
  LBAR();
  {
    u2 yv[8];
    #pragma unroll
    for(int c=0;c<8;c++) yv[c]=*(LAS const u2*)((LAS char*)smem+c*AW+8*tid);
    char* ob=(char*)a.out+(size_t)(4*tid)*2304+d0*2;
    #pragma unroll
    for(int i=0;i<4;i++){
      const unsigned sel=(i&1)?0x07060302u:0x05040100u;
      u4 o={__builtin_amdgcn_perm(yv[1][i>>1],yv[0][i>>1],sel),__builtin_amdgcn_perm(yv[3][i>>1],yv[2][i>>1],sel),
            __builtin_amdgcn_perm(yv[5][i>>1],yv[4][i>>1],sel),__builtin_amdgcn_perm(yv[7][i>>1],yv[6][i>>1],sel)};
      *(u4*)(ob+i*2304)=o;
    }
  }
  LBAR();
  TS(5);
}

__global__ __launch_bounds__(512,2) void fused_kernel(Args a){
  extern __shared__ __attribute__((aligned(16))) char smem[];
  TSINIT;
  TS(0);
  const int b=blockIdx.x, xcd=b&7, idx=b>>3;
  const unsigned gen=__hip_atomic_load((gu32*)(a.ws+WS_GEN),RLX_AGENT);
  if(idx<16) kv_block(a,smem,xcd,idx,gen,g_tsv);
  else if(idx<22) q_block(a,smem,2*xcd+(idx-16)/3,(idx-16)%3,gen,g_tsv);
  else if(idx<31) a_block(a,smem,xcd*9+(idx-22),g_tsv);
  const unsigned tag=mktag(gen);
  if(threadIdx.x==0) __hip_atomic_store((gu32*)(a.ws+WS_STARTED)+b,tag,RLX_AGENT);
  g2_stage(a,smem,4*xcd+(idx&3),idx>>2,tag,idx<16,g_tsv);
  if(b==248 && threadIdx.x<64){
    gu32* st=(gu32*)(a.ws+WS_STARTED)+threadIdx.x;
    #pragma unroll
    for(int i=0;i<4;i++) poll_flags(st+64*i,tag);
    if(threadIdx.x==0) __hip_atomic_store((gu32*)(a.ws+WS_GEN),tag,RLX_AGENT);
  }
  TS(11);
  TSFLUSH;
}

static inline void launch_fused(void* const* d_in, void* d_out, void* d_ws, hipStream_t stream){
  Args a{(const u16*)d_in[0],(const u16*)d_in[1],(const u16*)d_in[2],(const u16*)d_in[3],(const u16*)d_in[4],(const u16*)d_in[5],(const u16*)d_in[6],
         (const u16*)d_in[7],(const u16*)d_in[8],(const u16*)d_in[9],(u16*)d_out,(char*)d_ws};
  hipFuncSetAttribute((const void*)fused_kernel,hipFuncAttributeMaxDynamicSharedMemorySize,LDS_TOTAL);
  fused_kernel<<<256,512,LDS_TOTAL,stream>>>(a);
}

extern "C" void kernel_launch(void* const* d_in, const int* in_sizes, int n_in,
                              void* d_out, int out_size, void* d_ws, size_t ws_size,
                              hipStream_t stream) {
  launch_fused(d_in, d_out, d_ws, stream);
}
```
